```python
import torch

def _softmax(x, axis=-1):
    return torch.softmax(x, dim=axis)
def _gelu(x):
    return torch.nn.functional.gelu(x, approximate="tanh")
def _silu(x):
    return torch.nn.functional.silu(x)
def _sigmoid(x):
    return torch.sigmoid(x)
def _layernorm(x, axis=-1, eps=1e-5):
    m = x.mean(dim=axis, keepdim=True)
    v = x.var(dim=axis, keepdim=True, unbiased=False)
    return (x - m) / torch.sqrt(v + eps)
def _scatter_add(tgt, idx, src, axis=0):
    return tgt.clone().index_add_(axis, idx, src)
def _segment_sum(src, seg, n):
    out = torch.zeros((n,) + src.shape[1:], dtype=src.dtype)
    return out.index_add_(0, seg, src)
def _pad(x, pw):
    flat = tuple(v for pair in reversed(pw) for v in pair)
    return torch.nn.functional.pad(x, flat)

kernel_name = 'concat_heads__sliding_block_cached__sliding_block_cached__decode'

def setup_inputs():

    _w = (lambda a: (lambda t: t.to(torch.bfloat16) if t.is_floating_point() else t)(torch.as_tensor(a)))
    def _A_setup():
        B, Sq, D = 1, 1, 512
        Hq, Hkv, Sk, Dh = 32, 8, 32768, 128
        assert Hq % Hkv == 0, (Hq, Hkv)
        return {
            "x": _w((lambda *s: torch.randn(*s, dtype=torch.bfloat16) * 0.8)(B, Sq, D)),
            "k_cache": _w((lambda *s: torch.randn(*s, dtype=torch.bfloat16) * 0.8)(B, Hkv, Sk, Dh)),
            "v_cache": _w((lambda *s: torch.randn(*s, dtype=torch.bfloat16) * 0.05)(B, Hkv, Sk, Dh)),
            "wq": _w((lambda *s: torch.randn(*s, dtype=torch.bfloat16) * 0.8)(D, Hq * Dh) * (D ** -0.5)),
            "wo": _w((lambda *s: torch.randn(*s, dtype=torch.bfloat16) * 0.05)(Hq * Dh, D) * ((Hq * Dh) ** -0.5)),
        }
    _A_in = _A_setup()

    def _B_setup():
        B, Sq, D = 1, 1, 512
        Hq, Hkv, Sk, Dh = 32, 8, 32768, 128
        assert Hq % Hkv == 0, (Hq, Hkv)
        return {
            "x": _w((lambda *s: torch.randn(*s, dtype=torch.bfloat16) * 0.8)(B, Sq, D)),
            "k_cache": _w((lambda *s: torch.randn(*s, dtype=torch.bfloat16) * 0.8)(B, Hkv, Sk, Dh)),
            "v_cache": _w((lambda *s: torch.randn(*s, dtype=torch.bfloat16) * 0.05)(B, Hkv, Sk, Dh)),
            "wq": _w((lambda *s: torch.randn(*s, dtype=torch.bfloat16) * 0.8)(D, Hq * Dh) * (D ** -0.5)),
            "wo": _w((lambda *s: torch.randn(*s, dtype=torch.bfloat16) * 0.05)(Hq * Dh, D) * ((Hq * Dh) ** -0.5)),
        }
    _B_in = _B_setup()

    return {
        "A_x": _A_in["x"],
        "A_k_cache": _A_in["k_cache"],
        "A_v_cache": _A_in["v_cache"],
        "A_wq": _A_in["wq"],
        "A_wo": _A_in["wo"],
        "B_x": _B_in["x"],
        "B_k_cache": _B_in["k_cache"],
        "B_v_cache": _B_in["v_cache"],
        "B_wq": _B_in["wq"],
        "B_wo": _B_in["wo"],
    }

def reference(A_x, A_k_cache, A_v_cache, A_wq, A_wo, B_x, B_k_cache, B_v_cache, B_wq, B_wo):
    def _A_ref(x, k_cache, v_cache, wq, wo):
        B, Sq, D = x.shape
        Hq, Dh = 32, 128
        Hkv, Sk = k_cache.shape[1], k_cache.shape[2]
        W = min(4096, max(256, Sk // 4))
        rep = Hq // Hkv
        q = (lambda x, *d: x.permute(*d))((x @ wq).reshape(B, Sq, Hq, Dh), 0, 2, 1, 3)
        k = (lambda x, n, axis: torch.repeat_interleave(x, n, dim=axis))(k_cache[:, :, Sk - W:, :], rep, axis=1)
        v = (lambda x, n, axis: torch.repeat_interleave(x, n, dim=axis))(v_cache[:, :, Sk - W:, :], rep, axis=1)
        scores = torch.einsum("bhqd,bhkd->bhqk", q, k) * (Dh ** -0.5)
        p = _softmax(scores, axis=-1)
        o = torch.einsum("bhqk,bhkd->bhqd", p, v)
        return (lambda x, *d: x.permute(*d))(o, 0, 2, 1, 3).reshape(B, Sq, Hq * Dh) @ wo

    def _B_ref(x, k_cache, v_cache, wq, wo):
        B, Sq, D = x.shape
        Hq, Dh = 32, 128
        Hkv, Sk = k_cache.shape[1], k_cache.shape[2]
        W = min(4096, max(256, Sk // 4))
        rep = Hq // Hkv
        q = (lambda x, *d: x.permute(*d))((x @ wq).reshape(B, Sq, Hq, Dh), 0, 2, 1, 3)
        k = (lambda x, n, axis: torch.repeat_interleave(x, n, dim=axis))(k_cache[:, :, Sk - W:, :], rep, axis=1)
        v = (lambda x, n, axis: torch.repeat_interleave(x, n, dim=axis))(v_cache[:, :, Sk - W:, :], rep, axis=1)
        scores = torch.einsum("bhqd,bhkd->bhqk", q, k) * (Dh ** -0.5)
        p = _softmax(scores, axis=-1)
        o = torch.einsum("bhqk,bhkd->bhqd", p, v)
        return (lambda x, *d: x.permute(*d))(o, 0, 2, 1, 3).reshape(B, Sq, Hq * Dh) @ wo

    a = _A_ref(A_x, A_k_cache, A_v_cache, A_wq, A_wo)
    b = _B_ref(B_x, B_k_cache, B_v_cache, B_wq, B_wo)
    return torch.concatenate([a, b], axis=-1)
```

```cpp
#include <hip/hip_runtime.h>
#include <hip/hip_bf16.h>

typedef unsigned v4u __attribute__((ext_vector_type(4)));
typedef unsigned v2u __attribute__((ext_vector_type(2)));
typedef float f32x4 __attribute__((ext_vector_type(4)));
typedef float f32x2 __attribute__((ext_vector_type(2)));
typedef __bf16 bf16x8 __attribute__((ext_vector_type(8)));
typedef __bf16 bf16x2 __attribute__((ext_vector_type(2)));
#define LAS __attribute__((address_space(3)))
#define GAS __attribute__((address_space(1)))

#define KV_AUX 2
#define W_AUX 2
constexpr int AUX_SC1 = 16;
constexpr int RSRC_W3 = 0x00020000;

constexpr int SK = 32768, WIN = 4096, DH = 128, DM = 512, NQ = 4096;
constexpr int NSPLIT = 16, KEYS_WG = WIN / NSPLIT;
constexpr int NSW = 8;
constexpr int NTHR = (NSW + 1) * 64;
constexpr float SM_SCALE_LOG2 = 0.088388347648318440f * 1.4426950408889634f;

constexpr unsigned WS_EPOCH = 0;
constexpr unsigned WS_Q = 4096;
constexpr unsigned WS_ML = WS_Q + 16 * 16 * 128;
constexpr unsigned WS_O = WS_ML + 16 * 16 * 128;
constexpr unsigned WS_Y = WS_O + 16 * 16 * 4096;
constexpr unsigned WS_END = WS_Y + 2 * 128 * 4096;

constexpr int L_QPART = 0;
constexpr int L_QFULL = 1024;
constexpr int QF_STRIDE = 320;
constexpr int L_MX = L_QFULL + 4 * QF_STRIDE;
constexpr int L_LS = L_MX + 128;
constexpr int L_OFIN = L_LS + 128;
constexpr int L_TAG = L_OFIN + 64;
constexpr int L_OPART = L_TAG + 16 + 48;
constexpr int OP_HS = 1152, OP_WS = 2304;
constexpr int L_USED = L_OPART + NSW * OP_WS;
constexpr int LDS_BYTES = 84 * 1024;
static_assert(L_USED <= LDS_BYTES && (L_OPART % 16) == 0 && (L_MX % 16) == 0, "lds map");

constexpr unsigned SPIN_CAP = 1u << 16;

__device__ __forceinline__ float dot2(unsigned a, unsigned b, float c) {
    return __builtin_amdgcn_fdot2_f32_bf16(__builtin_bit_cast(bf16x2, a), __builtin_bit_cast(bf16x2, b), c, false);
}
__device__ __forceinline__ unsigned pack_bf16(float lo, float hi) {
    bf16x2 v = {(__bf16)lo, (__bf16)hi};
    return __builtin_bit_cast(unsigned, v);
}
__device__ __forceinline__ unsigned perm_lo(unsigned a, unsigned b) { return __builtin_amdgcn_perm(b, a, 0x05040100u); }
__device__ __forceinline__ unsigned perm_hi(unsigned a, unsigned b) { return __builtin_amdgcn_perm(b, a, 0x07060302u); }
template <int CTRL> __device__ __forceinline__ float dpp(float x) {
    return __builtin_bit_cast(float, __builtin_amdgcn_mov_dpp(__builtin_bit_cast(int, x), CTRL, 0xf, 0xf, true));
}
constexpr int XOR2 = 0x4E, ROR4 = 0x124, ROR8 = 0x128;
__device__ __forceinline__ float xrow_max(float x) {
    auto s = __builtin_amdgcn_permlane16_swap(__float_as_uint(x), __float_as_uint(x), false, false);
    x = fmaxf(__uint_as_float(s[0]), __uint_as_float(s[1]));
    auto t = __builtin_amdgcn_permlane32_swap(__float_as_uint(x), __float_as_uint(x), false, false);
    return fmaxf(__uint_as_float(t[0]), __uint_as_float(t[1]));
}
__device__ __forceinline__ float xrow_sum(float x) {
    auto s = __builtin_amdgcn_permlane16_swap(__float_as_uint(x), __float_as_uint(x), false, false);
    x = __uint_as_float(s[0]) + __uint_as_float(s[1]);
    auto t = __builtin_amdgcn_permlane32_swap(__float_as_uint(x), __float_as_uint(x), false, false);
    return __uint_as_float(t[0]) + __uint_as_float(t[1]);
}
__device__ __forceinline__ f32x4 mfma16(v4u a, v4u b, f32x4 c) {
    return __builtin_amdgcn_mfma_f32_16x16x32_bf16(__builtin_bit_cast(bf16x8, a), __builtin_bit_cast(bf16x8, b), c, 0, 0, 0);
}
#define SB() __builtin_amdgcn_sched_barrier(0)
#define BAR() do { asm volatile("s_waitcnt lgkmcnt(0)" ::: "memory"); __builtin_amdgcn_s_barrier(); asm volatile("" ::: "memory"); SB(); } while (0)
#define VMWAIT(n) do { SB(); asm volatile("s_waitcnt vmcnt(" #n ")" ::: "memory"); SB(); } while (0)

#define STAMP(i) do { } while (0)

__global__ void __launch_bounds__(NTHR, 1)
fused_decode_kernel(const unsigned short* __restrict__ xA, const unsigned short* __restrict__ kA, const unsigned short* __restrict__ vA,
                    const unsigned short* __restrict__ wqA, const unsigned short* __restrict__ woA,
                    const unsigned short* __restrict__ xB, const unsigned short* __restrict__ kB, const unsigned short* __restrict__ vB,
                    const unsigned short* __restrict__ wqB, const unsigned short* __restrict__ woB,
                    unsigned short* __restrict__ out, unsigned char* __restrict__ ws)
{
    extern __shared__ __attribute__((aligned(16))) unsigned char smem[];
    LAS unsigned char* L = (LAS unsigned char*)smem;
    const int tid = threadIdx.x, lane = tid & 63;
    const int w = __builtin_amdgcn_readfirstlane(tid >> 6);
    const int bid = blockIdx.x;
    const int kvh = bid & 7, b = (bid >> 3) & 1, s = bid >> 4;
    const int grp = b * 8 + kvh;
    const int g = lane >> 4, c = lane & 15;

    STAMP(0);
    const unsigned short* x = b ? xB : xA;
    const unsigned char* kp = (const unsigned char*)(b ? kB : kA) + ((size_t)kvh * SK + (SK - WIN) + KEYS_WG * s) * (DH * 2);
    const unsigned char* vp = (const unsigned char*)(b ? vB : vA) + ((size_t)kvh * SK + (SK - WIN) + KEYS_WG * s) * (DH * 2);
    const unsigned char* wqp = (const unsigned char*)(b ? wqB : wqA) + (kvh * 512 + 32 * s) * 2;
    const unsigned char* wop = (const unsigned char*)(b ? woB : woA) + (size_t)(kvh * 512 + 32 * s) * (DM * 2);
    asm volatile("" : "+s"(x), "+s"(kp), "+s"(vp), "+s"(wqp), "+s"(wop), "+s"(out), "+s"(ws));
    const __amdgpu_buffer_rsrc_t rws = __builtin_amdgcn_make_buffer_rsrc((void*)ws, 0, (int)WS_END, RSRC_W3);

    if (w < NSW) {
        const __amdgpu_buffer_rsrc_t rx = __builtin_amdgcn_make_buffer_rsrc((void*)x, 0, DM * 2, RSRC_W3);
        const __amdgpu_buffer_rsrc_t rk = __builtin_amdgcn_make_buffer_rsrc((void*)kp, 0, KEYS_WG * DH * 2, RSRC_W3);
        const __amdgpu_buffer_rsrc_t rv = __builtin_amdgcn_make_buffer_rsrc((void*)vp, 0, KEYS_WG * DH * 2, RSRC_W3);
        const __amdgpu_buffer_rsrc_t rq = __builtin_amdgcn_make_buffer_rsrc((void*)wqp, 0, (DM - 1) * NQ * 2 + 64, RSRC_W3);
        const __amdgpu_buffer_rsrc_t ro = __builtin_amdgcn_make_buffer_rsrc((void*)wop, 0, 32 * DM * 2, RSRC_W3);

        const int qrow = 64 * w + 4 * (lane >> 2);
        v4u wq[4];
        const int voq = qrow * (NQ * 2) + (lane & 3) * 16;
        const v2u xw = __builtin_amdgcn_raw_buffer_load_b64(rx, qrow * 2, 0, 0);
#pragma unroll
        for (int i = 0; i < 4; ++i) wq[i] = __builtin_amdgcn_raw_buffer_load_b128(rq, voq, i * (NQ * 2), W_AUX);
        SB();
        v4u kf[2][4];
        const int vok = (32 * w + c) * 256 + g * 16;
#pragma unroll
        for (int t = 0; t < 4; ++t) kf[0][t] = __builtin_amdgcn_raw_buffer_load_b128(rk, vok + t * 64, 0, KV_AUX);
        SB();
        float a[8];
#pragma unroll
        for (int e = 0; e < 8; ++e) a[e] = 0.f;
#pragma unroll
        for (int k = 0; k < 4; ++k) {
            a[2 * k] = dot2(perm_lo(wq[0][k], wq[1][k]), xw[0], a[2 * k]);
            a[2 * k + 1] = dot2(perm_hi(wq[0][k], wq[1][k]), xw[0], a[2 * k + 1]);
            a[2 * k] = dot2(perm_lo(wq[2][k], wq[3][k]), xw[1], a[2 * k]);
            a[2 * k + 1] = dot2(perm_hi(wq[2][k], wq[3][k]), xw[1], a[2 * k + 1]);
        }
        STAMP(1);
        float r4[4], r2[2];
#pragma unroll
        for (int e = 0; e < 4; ++e) {
            auto t = __builtin_amdgcn_permlane32_swap(__float_as_uint(a[e]), __float_as_uint(a[e + 4]), false, false);
            r4[e] = __uint_as_float(t[0]) + __uint_as_float(t[1]);
        }
#pragma unroll
        for (int e = 0; e < 2; ++e) {
            auto t = __builtin_amdgcn_permlane16_swap(__float_as_uint(r4[e]), __float_as_uint(r4[e + 2]), false, false);
            float v = __uint_as_float(t[0]) + __uint_as_float(t[1]);
            v += dpp<ROR4>(v);
            v += dpp<ROR8>(v);
            r2[e] = v;
        }
        if ((lane & 12) == 0) *(LAS f32x2*)(L + L_QPART + w * 128 + ((lane & 3) * 8 + 2 * g) * 4) = f32x2{r2[0], r2[1]};
        BAR();
        const unsigned tag = __builtin_amdgcn_readfirstlane(*(LAS unsigned*)(L + L_TAG));
        BAR();
        STAMP(2);
#pragma unroll
        for (int t = 0; t < 4; ++t) kf[1][t] = __builtin_amdgcn_raw_buffer_load_b128(rk, vok + t * 64, 4096, KV_AUX);
        SB();
        v4u vf[8];
        const int vov = (32 * w + 4 * g) * 256 + c * 16;
        VMWAIT(4);
#pragma unroll
        for (int j = 0; j < 4; ++j) vf[j] = __builtin_amdgcn_raw_buffer_load_b128(rv, vov + j * 256, 0, KV_AUX);
        VMWAIT(4);
#pragma unroll
        for (int j = 0; j < 4; ++j) vf[4 + j] = __builtin_amdgcn_raw_buffer_load_b128(rv, vov + j * 256, 4096, KV_AUX);
        SB();
        STAMP(3);
        BAR();
        STAMP(4);
        v4u qb[4];
#pragma unroll
        for (int t = 0; t < 4; ++t) qb[t] = *(LAS v4u*)(L + L_QFULL + (c >> 2) * QF_STRIDE + (32 * t + 8 * g) * 2);
        f32x4 sc[2];
#pragma unroll
        for (int u = 0; u < 2; ++u) {
            sc[u] = f32x4{0.f, 0.f, 0.f, 0.f};
#pragma unroll
            for (int t = 0; t < 4; ++t) sc[u] = mfma16(kf[u][t], qb[t], sc[u]);
        }
        float m = fmaxf(fmaxf(fmaxf(sc[0][0], sc[0][1]), fmaxf(sc[0][2], sc[0][3])), fmaxf(fmaxf(sc[1][0], sc[1][1]), fmaxf(sc[1][2], sc[1][3])));
        m = xrow_max(m);
        if ((lane & 0x33) == 0) *(LAS float*)(L + L_MX + ((c >> 2) * 8 + w) * 4) = m;
        BAR();
        const f32x4 m0 = *(LAS f32x4*)(L + L_MX + (c >> 2) * 32), m1 = *(LAS f32x4*)(L + L_MX + (c >> 2) * 32 + 16);
        const float mw = fmaxf(fmaxf(fmaxf(m0[0], m0[1]), fmaxf(m0[2], m0[3])), fmaxf(fmaxf(m1[0], m1[1]), fmaxf(m1[2], m1[3]))) * SM_SCALE_LOG2;
        float p[8];
#pragma unroll
        for (int u = 0; u < 2; ++u)
#pragma unroll
            for (int r = 0; r < 4; ++r) p[4 * u + r] = __builtin_amdgcn_exp2f(fmaf(sc[u][r], SM_SCALE_LOG2, -mw));
        float ls = ((p[0] + p[1]) + (p[2] + p[3])) + ((p[4] + p[5]) + (p[6] + p[7]));
        ls = xrow_sum(ls);
        if ((lane & 0x33) == 0) *(LAS float*)(L + L_LS + ((c >> 2) * 8 + w) * 4) = ls;
        const v4u pa = {pack_bf16(p[0], p[1]), pack_bf16(p[2], p[3]), pack_bf16(p[4], p[5]), pack_bf16(p[6], p[7])};
        STAMP(5);
        v2u wo[8];
        const int voo = g * 8192 + w * 128 + c * 8;
        VMWAIT(4);
#pragma unroll
        for (int j = 0; j < 8; ++j) wo[j] = __builtin_amdgcn_raw_buffer_load_b64(ro, voo + (j & 3) * 1024, (j >> 2) * 4096, W_AUX);
        SB();
        f32x4 oacc[8];
#pragma unroll
        for (int i = 0; i < 8; ++i) {
            v4u bv;
#pragma unroll
            for (int jj = 0; jj < 4; ++jj)
                bv[jj] = (i & 1) ? perm_hi(vf[2 * jj][i >> 1], vf[2 * jj + 1][i >> 1]) : perm_lo(vf[2 * jj][i >> 1], vf[2 * jj + 1][i >> 1]);
            oacc[i] = mfma16(pa, bv, f32x4{0.f, 0.f, 0.f, 0.f});
        }
        *(LAS f32x4*)(L + L_OPART + w * OP_WS + lane * 16) = f32x4{oacc[0][0], oacc[1][0], oacc[2][0], oacc[3][0]};
        *(LAS f32x4*)(L + L_OPART + w * OP_WS + OP_HS + lane * 16) = f32x4{oacc[4][0], oacc[5][0], oacc[6][0], oacc[7][0]};
        STAMP(6);
        BAR();
        if (w < 4) {
            const int t = tid;
            const int off = ((t & 3) >> 1) * OP_HS + (t >> 2) * 16 + (t & 1) * 8;
            f32x2 v = {0.f, 0.f};
#pragma unroll
            for (int ww = 0; ww < NSW; ++ww) v += *(LAS f32x2*)(L + L_OPART + ww * OP_WS + off);
            const v4u unit = {__float_as_uint(v[0]), tag, __float_as_uint(v[1]), tag};
            __builtin_amdgcn_raw_buffer_store_b128(unit, rws, WS_O + (grp * 16 + s) * 4096 + t * 16, 0, AUX_SC1);
        } else if (w == 4) {
            if (lane < 4) {
                const f32x4 a0 = *(LAS f32x4*)(L + L_MX + lane * 32), a1 = *(LAS f32x4*)(L + L_MX + lane * 32 + 16);
                const f32x4 l0 = *(LAS f32x4*)(L + L_LS + lane * 32), l1 = *(LAS f32x4*)(L + L_LS + lane * 32 + 16);
                const float mm = fmaxf(fmaxf(fmaxf(a0[0], a0[1]), fmaxf(a0[2], a0[3])), fmaxf(fmaxf(a1[0], a1[1]), fmaxf(a1[2], a1[3]))) * SM_SCALE_LOG2;
                const float ll = ((l0[0] + l0[1]) + (l0[2] + l0[3])) + ((l1[0] + l1[1]) + (l1[2] + l1[3]));
                const v4u unit = {__float_as_uint(mm), tag, __float_as_uint(ll), tag};
                __builtin_amdgcn_raw_buffer_store_b128(unit, rws, WS_ML + (grp * 16 + s) * 128 + lane * 16, 0, AUX_SC1);
            }
        }
        STAMP(7);
        v4u wb[4];
#pragma unroll
        for (int i = 0; i < 4; ++i)
#pragma unroll
            for (int jj = 0; jj < 4; ++jj)
                wb[i][jj] = (i & 1) ? perm_hi(wo[2 * jj][i >> 1], wo[2 * jj + 1][i >> 1]) : perm_lo(wo[2 * jj][i >> 1], wo[2 * jj + 1][i >> 1]);
        asm volatile("" : "+v"(wb[0]), "+v"(wb[1]), "+v"(wb[2]), "+v"(wb[3]));
        STAMP(8);
        BAR();
        STAMP(9);
        const v4u oa = *(LAS v4u*)(L + L_OFIN + g * 16);
        f32x4 y[4];
#pragma unroll
        for (int i = 0; i < 4; ++i) y[i] = mfma16(oa, wb[i], f32x4{0.f, 0.f, 0.f, 0.f});
        if (g < 2) {
            const float v0 = g ? y[2][0] : y[0][0], v1 = g ? y[3][0] : y[1][0];
            const v4u unit = {__float_as_uint(v0), tag, __float_as_uint(v1), tag};
            __builtin_amdgcn_raw_buffer_store_b128(unit, rws, WS_Y + (b * 128 + kvh * 16 + s) * 4096 + (32 * w + 2 * c + g) * 16, 0, AUX_SC1);
        }
        STAMP(10);
    } else {
        const unsigned tag = __builtin_amdgcn_readfirstlane(
            __hip_atomic_load((GAS unsigned*)(ws + WS_EPOCH + b * 256), __ATOMIC_RELAXED, __HIP_MEMORY_SCOPE_AGENT)) + 1u;
        if (lane == 0) *(LAS unsigned*)(L + L_TAG) = tag;
        BAR();
        {
            f32x4 v = {0.f, 0.f, 0.f, 0.f};
            const int ll = lane & 7;
#pragma unroll
            for (int ww = 0; ww < NSW; ++ww) v += *(LAS f32x4*)(L + L_QPART + ww * 128 + ll * 16);
            const v4u unit = {pack_bf16(v[0], v[1]), tag, pack_bf16(v[2], v[3]), tag};
            if (lane < 8) __builtin_amdgcn_raw_buffer_store_b128(unit, rws, WS_Q + (grp * 16 + s) * 128 + lane * 16, 0, AUX_SC1);
        }
        SB();
        BAR();
        STAMP(1);
        {
            v4u u0, u1;
            const int off = WS_Q + grp * 2048 + lane * 16;
            for (unsigned spin = 0; spin < SPIN_CAP; ++spin) {
                asm volatile("" ::: "memory");
                u0 = __builtin_amdgcn_raw_buffer_load_b128(rws, off, 0, AUX_SC1);
                u1 = __builtin_amdgcn_raw_buffer_load_b128(rws, off + 1024, 0, AUX_SC1);
                const bool ok = (u0[1] == tag) & (u0[3] == tag) & (u1[1] == tag) & (u1[3] == tag);
                if (__all(ok)) break;
            }
            const int p0 = lane >> 3, p1 = 8 + p0;
            *(LAS v2u*)(L + L_QFULL + (p0 >> 2) * QF_STRIDE + (32 * (p0 & 3) + 4 * (lane & 7)) * 2) = v2u{u0[0], u0[2]};
            *(LAS v2u*)(L + L_QFULL + (p1 >> 2) * QF_STRIDE + (32 * (p1 & 3) + 4 * (lane & 7)) * 2) = v2u{u1[0], u1[2]};
        }
        STAMP(2);
        BAR();
        BAR();
        BAR();
        STAMP(3);
        {
            v4u uo[4], um[4];
            const int offo = WS_O + grp * 65536 + g * 4096 + (16 * s + c) * 16;
            const int offm = WS_ML + grp * 2048 + g * 128 + (s >> 2) * 16;
            for (unsigned spin = 0; spin < SPIN_CAP; ++spin) {
                asm volatile("" ::: "memory");
                bool ok = true;
#pragma unroll
                for (int k = 0; k < 4; ++k) {
                    uo[k] = __builtin_amdgcn_raw_buffer_load_b128(rws, offo, k * 16384, AUX_SC1);
                    um[k] = __builtin_amdgcn_raw_buffer_load_b128(rws, offm, k * 512, AUX_SC1);
                }
#pragma unroll
                for (int k = 0; k < 4; ++k) ok &= (uo[k][1] == tag) & (uo[k][3] == tag) & (um[k][1] == tag) & (um[k][3] == tag);
                if (__all(ok)) break;
            }
            STAMP(4);
            float M = fmaxf(fmaxf(__uint_as_float(um[0][0]), __uint_as_float(um[1][0])), fmaxf(__uint_as_float(um[2][0]), __uint_as_float(um[3][0])));
            M = xrow_max(M);
            float n0 = 0.f, n1 = 0.f, den = 0.f;
#pragma unroll
            for (int k = 0; k < 4; ++k) {
                const float wt = __builtin_amdgcn_exp2f(__uint_as_float(um[k][0]) - M);
                n0 = fmaf(wt, __uint_as_float(uo[k][0]), n0);
                n1 = fmaf(wt, __uint_as_float(uo[k][2]), n1);
                den = fmaf(wt, __uint_as_float(um[k][2]), den);
            }
            n0 = xrow_sum(n0); n1 = xrow_sum(n1); den = xrow_sum(den);
            const float inv = 1.0f / den;
            if (lane < 16) *(LAS unsigned*)(L + L_OFIN + lane * 4) = pack_bf16(n0 * inv, n1 * inv);
        }
        BAR();
        STAMP(5);
        {
            const int idx = kvh * 16 + s;
            v4u uy[4];
            const int offy = WS_Y + b * (128 * 4096) + (lane >> 1) * 4096 + (2 * idx + (lane & 1)) * 16;
            for (unsigned spin = 0; spin < SPIN_CAP; ++spin) {
                asm volatile("" ::: "memory");
                bool ok = true;
#pragma unroll
                for (int k = 0; k < 4; ++k) uy[k] = __builtin_amdgcn_raw_buffer_load_b128(rws, offy, k * (32 * 4096), AUX_SC1);
#pragma unroll
                for (int k = 0; k < 4; ++k) ok &= (uy[k][1] == tag) & (uy[k][3] == tag);
                if (__all(ok)) break;
            }
            STAMP(6);
            float y0 = (__uint_as_float(uy[0][0]) + __uint_as_float(uy[1][0])) + (__uint_as_float(uy[2][0]) + __uint_as_float(uy[3][0]));
            float y1 = (__uint_as_float(uy[0][2]) + __uint_as_float(uy[1][2])) + (__uint_as_float(uy[2][2]) + __uint_as_float(uy[3][2]));
            y0 += dpp<XOR2>(y0); y1 += dpp<XOR2>(y1);
            y0 += dpp<ROR4>(y0); y1 += dpp<ROR4>(y1);
            y0 += dpp<ROR8>(y0); y1 += dpp<ROR8>(y1);
            y0 = xrow_sum(y0); y1 = xrow_sum(y1);
            if (lane < 2) ((unsigned*)out)[(b * 512 + 4 * idx) / 2 + lane] = pack_bf16(y0, y1);
            if (idx == 0 && lane == 0) __hip_atomic_store((GAS unsigned*)(ws + WS_EPOCH + b * 256), tag, __ATOMIC_RELAXED, __HIP_MEMORY_SCOPE_AGENT);
        }
        STAMP(7);
    }
}

extern "C" void kernel_launch(void* const* d_in, const int* in_sizes, int n_in, void* d_out, int out_size, void* d_ws, size_t ws_size,
                              hipStream_t stream) {
    (void)in_sizes; (void)n_in; (void)out_size; (void)ws_size;
    typedef const unsigned short* P;
    (void)hipFuncSetAttribute((const void*)fused_decode_kernel, hipFuncAttributeMaxDynamicSharedMemorySize, LDS_BYTES);
    fused_decode_kernel<<<256, NTHR, LDS_BYTES, stream>>>((P)d_in[0], (P)d_in[1], (P)d_in[2], (P)d_in[3], (P)d_in[4],
                                                         (P)d_in[5], (P)d_in[6], (P)d_in[7], (P)d_in[8], (P)d_in[9],
                                                         (unsigned short*)d_out, (unsigned char*)d_ws);
}
```

```cpp
#include <hip/hip_runtime.h>
#include <hip/hip_bf16.h>

typedef unsigned v4u __attribute__((ext_vector_type(4)));
typedef unsigned v2u __attribute__((ext_vector_type(2)));
typedef float f32x4 __attribute__((ext_vector_type(4)));
typedef float f32x2 __attribute__((ext_vector_type(2)));
typedef __bf16 bf16x8 __attribute__((ext_vector_type(8)));
typedef __bf16 bf16x2 __attribute__((ext_vector_type(2)));
#define LAS __attribute__((address_space(3)))
#define GAS __attribute__((address_space(1)))

constexpr int AUX_NT = 2;
#define KV_MOD "nt"
constexpr int AUX_SC1 = 16;
constexpr int RSRC_W3 = 0x00020000;

constexpr int SK = 32768, WIN = 4096, DH = 128, DM = 512, NQ = 4096;
constexpr int NSPLIT = 16, KEYS_WG = WIN / NSPLIT;
constexpr int NWW = 4, WCTL = 4, WKV0 = 5, NKV = 8;
constexpr int NTHR = (NWW + 1 + NKV) * 64;
constexpr float SM_SCALE_LOG2 = 0.088388347648318440f * 1.4426950408889634f;

constexpr unsigned WS_EPOCH = 0;
constexpr unsigned WS_Q = 4096;
constexpr unsigned WS_ML = WS_Q + 16 * 16 * 512;
constexpr unsigned WS_O = WS_ML + 16 * 16 * 128;
constexpr unsigned WS_Y = WS_O + 16 * 16 * 2048;
constexpr unsigned WS_END = WS_Y + 2 * 128 * 2048;

constexpr int L_SYNC = 0;
constexpr int S_QP = 0, S_QPUB = 4, S_FQ = 8, S_ML = 12, S_O = 16, S_PUB = 20, S_FO = 24, S_TAG = 28;
constexpr int L_QPART = 64;
constexpr int L_QFULL = L_QPART + 2048;
constexpr int QF_STRIDE = 320;
constexpr int L_MX = L_QFULL + 4 * QF_STRIDE;
constexpr int L_LS = L_MX + 128;
constexpr int L_OFIN = L_LS + 128;
constexpr int L_OPART = L_OFIN + 64;
constexpr int OP_HS = 1152, OP_WS = 2304;
constexpr int L_USED = L_OPART + NKV * OP_WS;
constexpr int L_KT = 24576;
constexpr int LDS_BYTES = L_KT + 65536;
static_assert(L_USED <= L_KT && (L_OPART % 16) == 0 && (L_MX % 16) == 0 && (L_QFULL % 16) == 0, "lds map");

constexpr int Q_AT = 250;
constexpr int O_DELAY = 28;
constexpr int Y_DELAY = 28;

#define POLLQ_TEXT \
        "s_mov_b64 %[p0], -1\n\t" \
        "s_mov_b64 %[p1], -1\n\t" \
        "s_mov_b64 %[p2], -1\n\t" \
        "s_mov_b64 %[p3], -1\n\t" \
        "s_mov_b64 %[p4], -1\n\t" \
        "buffer_load_dwordx4 v[88:91], %[off], %[rs], 0 offen sc1\n\t" \
        "buffer_load_dwordx4 v[92:95], %[off], %[rs], %[s1] offen sc1\n\t" \
        "buffer_load_dwordx4 v[96:99], %[off], %[rs], %[s2] offen sc1\n\t" \
        "buffer_load_dwordx4 v[100:103], %[off1], %[rs], 0 offen sc1\n\t" \
        "buffer_load_dwordx4 v[104:107], %[off1], %[rs], 0 offen offset:1024 sc1\n\t" \
        "s_sleep 12\n\t" \
        "buffer_load_dwordx4 v[108:111], %[off], %[rs], 0 offen sc1\n\t" \
        "buffer_load_dwordx4 v[112:115], %[off], %[rs], %[s1] offen sc1\n\t" \
        "buffer_load_dwordx4 v[116:119], %[off], %[rs], %[s2] offen sc1\n\t" \
        "buffer_load_dwordx4 v[120:123], %[off1], %[rs], 0 offen sc1\n\t" \
        "buffer_load_dwordx4 v[124:127], %[off1], %[rs], 0 offen offset:1024 sc1\n\t" \
        "s_mov_b32 %[cnt], 32767\n\t" \
        "PL_%=:\n\t" \
        "s_waitcnt vmcnt(5)\n\t" \
        "s_mov_b64 exec, %[p0]\n\t" \
        "s_cbranch_execz PS0_0_%=\n\t" \
        "v_xor_b32 %[t], %[f0], v88\n\t" \
        "v_bitop3_b32 %[t], v89, %[f1], %[t] bitop3:0xbe\n\t" \
        "v_bitop3_b32 %[t], v90, %[f2], %[t] bitop3:0xbe\n\t" \
        "v_bitop3_b32 %[t], v91, %[f3], %[t] bitop3:0xbe\n\t" \
        "v_and_b32 %[t], 0xfff, %[t]\n\t" \
        "v_cmp_eq_u32 vcc, 0, %[t]\n\t" \
        "s_andn2_b64 %[p0], %[p0], vcc\n\t" \
        "s_mov_b64 exec, vcc\n\t" \
        "v_and_b32 %[o0], 0xfffff000, v88\n\t" \
        "v_and_b32 %[o1], 0xfffff000, v89\n\t" \
        "v_and_b32 %[o2], 0xfffff000, v90\n\t" \
        "v_and_b32 %[o3], 0xfffff000, v91\n\t" \
        "PS0_0_%=:\n\t" \
        "s_mov_b64 exec, %[p1]\n\t" \
        "s_cbranch_execz PS0_1_%=\n\t" \
        "v_xor_b32 %[t], %[f0], v92\n\t" \
        "v_bitop3_b32 %[t], v93, %[f1], %[t] bitop3:0xbe\n\t" \
        "v_bitop3_b32 %[t], v94, %[f2], %[t] bitop3:0xbe\n\t" \
        "v_bitop3_b32 %[t], v95, %[f3], %[t] bitop3:0xbe\n\t" \
        "v_and_b32 %[t], 0xfff, %[t]\n\t" \
        "v_cmp_eq_u32 vcc, 0, %[t]\n\t" \
        "s_andn2_b64 %[p1], %[p1], vcc\n\t" \
        "s_mov_b64 exec, vcc\n\t" \
        "v_and_b32 %[o4], 0xfffff000, v92\n\t" \
        "v_and_b32 %[o5], 0xfffff000, v93\n\t" \
        "v_and_b32 %[o6], 0xfffff000, v94\n\t" \
        "v_and_b32 %[o7], 0xfffff000, v95\n\t" \
        "PS0_1_%=:\n\t" \
        "s_mov_b64 exec, %[p2]\n\t" \
        "s_cbranch_execz PS0_2_%=\n\t" \
        "v_xor_b32 %[t], %[f0], v96\n\t" \
        "v_bitop3_b32 %[t], v97, %[f1], %[t] bitop3:0xbe\n\t" \
        "v_bitop3_b32 %[t], v98, %[f2], %[t] bitop3:0xbe\n\t" \
        "v_bitop3_b32 %[t], v99, %[f3], %[t] bitop3:0xbe\n\t" \
        "v_and_b32 %[t], 0xfff, %[t]\n\t" \
        "v_cmp_eq_u32 vcc, 0, %[t]\n\t" \
        "s_andn2_b64 %[p2], %[p2], vcc\n\t" \
        "s_mov_b64 exec, vcc\n\t" \
        "v_and_b32 %[o8], 0xfffff000, v96\n\t" \
        "v_and_b32 %[o9], 0xfffff000, v97\n\t" \
        "v_and_b32 %[o10], 0xfffff000, v98\n\t" \
        "v_and_b32 %[o11], 0xfffff000, v99\n\t" \
        "PS0_2_%=:\n\t" \
        "s_mov_b64 exec, %[p3]\n\t" \
        "s_cbranch_execz PS0_3_%=\n\t" \
        "v_xor_b32 %[t], %[f0], v100\n\t" \
        "v_bitop3_b32 %[t], v101, %[f1], %[t] bitop3:0xbe\n\t" \
        "v_bitop3_b32 %[t], v102, %[f2], %[t] bitop3:0xbe\n\t" \
        "v_bitop3_b32 %[t], v103, %[f3], %[t] bitop3:0xbe\n\t" \
        "v_and_b32 %[t], 0xfff, %[t]\n\t" \
        "v_cmp_eq_u32 vcc, 0, %[t]\n\t" \
        "s_andn2_b64 %[p3], %[p3], vcc\n\t" \
        "s_mov_b64 exec, vcc\n\t" \
        "v_and_b32 %[o12], 0xfffff000, v100\n\t" \
        "v_and_b32 %[o13], 0xfffff000, v101\n\t" \
        "v_and_b32 %[o14], 0xfffff000, v102\n\t" \
        "v_and_b32 %[o15], 0xfffff000, v103\n\t" \
        "PS0_3_%=:\n\t" \
        "s_mov_b64 exec, %[p4]\n\t" \
        "s_cbranch_execz PS0_4_%=\n\t" \
        "v_xor_b32 %[t], %[f0], v104\n\t" \
        "v_bitop3_b32 %[t], v105, %[f1], %[t] bitop3:0xbe\n\t" \
        "v_bitop3_b32 %[t], v106, %[f2], %[t] bitop3:0xbe\n\t" \
        "v_bitop3_b32 %[t], v107, %[f3], %[t] bitop3:0xbe\n\t" \
        "v_and_b32 %[t], 0xfff, %[t]\n\t" \
        "v_cmp_eq_u32 vcc, 0, %[t]\n\t" \
        "s_andn2_b64 %[p4], %[p4], vcc\n\t" \
        "s_mov_b64 exec, vcc\n\t" \
        "v_and_b32 %[o16], 0xfffff000, v104\n\t" \
        "v_and_b32 %[o17], 0xfffff000, v105\n\t" \
        "v_and_b32 %[o18], 0xfffff000, v106\n\t" \
        "v_and_b32 %[o19], 0xfffff000, v107\n\t" \
        "PS0_4_%=:\n\t" \
        "s_or_b64 vcc, %[p0], %[p1]\n\t" \
        "s_or_b64 vcc, vcc, %[p2]\n\t" \
        "s_or_b64 vcc, vcc, %[p3]\n\t" \
        "s_or_b64 vcc, vcc, %[p4]\n\t" \
        "s_cbranch_scc0 PE_%=\n\t" \
        "s_or_b64 exec, %[p0], 1\n\t" \
        "buffer_load_dwordx4 v[88:91], %[off], %[rs], 0 offen sc1\n\t" \
        "s_or_b64 exec, %[p1], 1\n\t" \
        "buffer_load_dwordx4 v[92:95], %[off], %[rs], %[s1] offen sc1\n\t" \
        "s_or_b64 exec, %[p2], 1\n\t" \
        "buffer_load_dwordx4 v[96:99], %[off], %[rs], %[s2] offen sc1\n\t" \
        "s_or_b64 exec, %[p3], 1\n\t" \
        "buffer_load_dwordx4 v[100:103], %[off1], %[rs], 0 offen sc1\n\t" \
        "s_or_b64 exec, %[p4], 1\n\t" \
        "buffer_load_dwordx4 v[104:107], %[off1], %[rs], 0 offen offset:1024 sc1\n\t" \
        "s_waitcnt vmcnt(5)\n\t" \
        "s_mov_b64 exec, %[p0]\n\t" \
        "s_cbranch_execz PS1_0_%=\n\t" \
        "v_xor_b32 %[t], %[f0], v108\n\t" \
        "v_bitop3_b32 %[t], v109, %[f1], %[t] bitop3:0xbe\n\t" \
        "v_bitop3_b32 %[t], v110, %[f2], %[t] bitop3:0xbe\n\t" \
        "v_bitop3_b32 %[t], v111, %[f3], %[t] bitop3:0xbe\n\t" \
        "v_and_b32 %[t], 0xfff, %[t]\n\t" \
        "v_cmp_eq_u32 vcc, 0, %[t]\n\t" \
        "s_andn2_b64 %[p0], %[p0], vcc\n\t" \
        "s_mov_b64 exec, vcc\n\t" \
        "v_and_b32 %[o0], 0xfffff000, v108\n\t" \
        "v_and_b32 %[o1], 0xfffff000, v109\n\t" \
        "v_and_b32 %[o2], 0xfffff000, v110\n\t" \
        "v_and_b32 %[o3], 0xfffff000, v111\n\t" \
        "PS1_0_%=:\n\t" \
        "s_mov_b64 exec, %[p1]\n\t" \
        "s_cbranch_execz PS1_1_%=\n\t" \
        "v_xor_b32 %[t], %[f0], v112\n\t" \
        "v_bitop3_b32 %[t], v113, %[f1], %[t] bitop3:0xbe\n\t" \
        "v_bitop3_b32 %[t], v114, %[f2], %[t] bitop3:0xbe\n\t" \
        "v_bitop3_b32 %[t], v115, %[f3], %[t] bitop3:0xbe\n\t" \
        "v_and_b32 %[t], 0xfff, %[t]\n\t" \
        "v_cmp_eq_u32 vcc, 0, %[t]\n\t" \
        "s_andn2_b64 %[p1], %[p1], vcc\n\t" \
        "s_mov_b64 exec, vcc\n\t" \
        "v_and_b32 %[o4], 0xfffff000, v112\n\t" \
        "v_and_b32 %[o5], 0xfffff000, v113\n\t" \
        "v_and_b32 %[o6], 0xfffff000, v114\n\t" \
        "v_and_b32 %[o7], 0xfffff000, v115\n\t" \
        "PS1_1_%=:\n\t" \
        "s_mov_b64 exec, %[p2]\n\t" \
        "s_cbranch_execz PS1_2_%=\n\t" \
        "v_xor_b32 %[t], %[f0], v116\n\t" \
        "v_bitop3_b32 %[t], v117, %[f1], %[t] bitop3:0xbe\n\t" \
        "v_bitop3_b32 %[t], v118, %[f2], %[t] bitop3:0xbe\n\t" \
        "v_bitop3_b32 %[t], v119, %[f3], %[t] bitop3:0xbe\n\t" \
        "v_and_b32 %[t], 0xfff, %[t]\n\t" \
        "v_cmp_eq_u32 vcc, 0, %[t]\n\t" \
        "s_andn2_b64 %[p2], %[p2], vcc\n\t" \
        "s_mov_b64 exec, vcc\n\t" \
        "v_and_b32 %[o8], 0xfffff000, v116\n\t" \
        "v_and_b32 %[o9], 0xfffff000, v117\n\t" \
        "v_and_b32 %[o10], 0xfffff000, v118\n\t" \
        "v_and_b32 %[o11], 0xfffff000, v119\n\t" \
        "PS1_2_%=:\n\t" \
        "s_mov_b64 exec, %[p3]\n\t" \
        "s_cbranch_execz PS1_3_%=\n\t" \
        "v_xor_b32 %[t], %[f0], v120\n\t" \
        "v_bitop3_b32 %[t], v121, %[f1], %[t] bitop3:0xbe\n\t" \
        "v_bitop3_b32 %[t], v122, %[f2], %[t] bitop3:0xbe\n\t" \
        "v_bitop3_b32 %[t], v123, %[f3], %[t] bitop3:0xbe\n\t" \
        "v_and_b32 %[t], 0xfff, %[t]\n\t" \
        "v_cmp_eq_u32 vcc, 0, %[t]\n\t" \
        "s_andn2_b64 %[p3], %[p3], vcc\n\t" \
        "s_mov_b64 exec, vcc\n\t" \
        "v_and_b32 %[o12], 0xfffff000, v120\n\t" \
        "v_and_b32 %[o13], 0xfffff000, v121\n\t" \
        "v_and_b32 %[o14], 0xfffff000, v122\n\t" \
        "v_and_b32 %[o15], 0xfffff000, v123\n\t" \
        "PS1_3_%=:\n\t" \
        "s_mov_b64 exec, %[p4]\n\t" \
        "s_cbranch_execz PS1_4_%=\n\t" \
        "v_xor_b32 %[t], %[f0], v124\n\t" \
        "v_bitop3_b32 %[t], v125, %[f1], %[t] bitop3:0xbe\n\t" \
        "v_bitop3_b32 %[t], v126, %[f2], %[t] bitop3:0xbe\n\t" \
        "v_bitop3_b32 %[t], v127, %[f3], %[t] bitop3:0xbe\n\t" \
        "v_and_b32 %[t], 0xfff, %[t]\n\t" \
        "v_cmp_eq_u32 vcc, 0, %[t]\n\t" \
        "s_andn2_b64 %[p4], %[p4], vcc\n\t" \
        "s_mov_b64 exec, vcc\n\t" \
        "v_and_b32 %[o16], 0xfffff000, v124\n\t" \
        "v_and_b32 %[o17], 0xfffff000, v125\n\t" \
        "v_and_b32 %[o18], 0xfffff000, v126\n\t" \
        "v_and_b32 %[o19], 0xfffff000, v127\n\t" \
        "PS1_4_%=:\n\t" \
        "s_or_b64 vcc, %[p0], %[p1]\n\t" \
        "s_or_b64 vcc, vcc, %[p2]\n\t" \
        "s_or_b64 vcc, vcc, %[p3]\n\t" \
        "s_or_b64 vcc, vcc, %[p4]\n\t" \
        "s_cbranch_scc0 PE_%=\n\t" \
        "s_or_b64 exec, %[p0], 1\n\t" \
        "buffer_load_dwordx4 v[108:111], %[off], %[rs], 0 offen sc1\n\t" \
        "s_or_b64 exec, %[p1], 1\n\t" \
        "buffer_load_dwordx4 v[112:115], %[off], %[rs], %[s1] offen sc1\n\t" \
        "s_or_b64 exec, %[p2], 1\n\t" \
        "buffer_load_dwordx4 v[116:119], %[off], %[rs], %[s2] offen sc1\n\t" \
        "s_or_b64 exec, %[p3], 1\n\t" \
        "buffer_load_dwordx4 v[120:123], %[off1], %[rs], 0 offen sc1\n\t" \
        "s_or_b64 exec, %[p4], 1\n\t" \
        "buffer_load_dwordx4 v[124:127], %[off1], %[rs], 0 offen offset:1024 sc1\n\t" \
        "s_sub_u32 %[cnt], %[cnt], 1\n\t" \
        "s_cbranch_scc0 PL_%=\n\t" \
        "PE_%=:\n\t" \
        "s_mov_b64 exec, -1\n\t" \
        ""
#define POLLQ_CLOB "v88", "v89", "v90", "v91", "v92", "v93", "v94", "v95", "v96", "v97", "v98", "v99", "v100", "v101", "v102", "v103", "v104", "v105", "v106", "v107", "v108", "v109", "v110", "v111", "v112", "v113", "v114", "v115", "v116", "v117", "v118", "v119", "v120", "v121", "v122", "v123", "v124", "v125", "v126", "v127"
#define POLLO_TEXT \
        "s_mov_b64 %[p0], -1\n\t" \
        "s_mov_b64 %[p1], -1\n\t" \
        "s_mov_b64 %[p2], -1\n\t" \
        "s_mov_b64 %[p3], -1\n\t" \
        "buffer_load_dwordx4 v[112:115], %[off], %[rs], 0 offen sc1\n\t" \
        "buffer_load_dwordx4 v[116:119], %[off], %[rs], %[s1] offen sc1\n\t" \
        "buffer_load_dwordx4 v[120:123], %[offm], %[rs], 0 offen sc1\n\t" \
        "buffer_load_dwordx4 v[124:127], %[offm], %[rs], 0 offen offset:1024 sc1\n\t" \
        "s_mov_b32 %[cnt], 32767\n\t" \
        "PL_%=:\n\t" \
        "s_waitcnt vmcnt(0)\n\t" \
        "s_mov_b64 exec, %[p0]\n\t" \
        "s_cbranch_execz PS0_0_%=\n\t" \
        "v_xor_b32 %[t], %[f0], v112\n\t" \
        "v_bitop3_b32 %[t], v113, %[f1], %[t] bitop3:0xbe\n\t" \
        "v_bitop3_b32 %[t], v114, %[f2], %[t] bitop3:0xbe\n\t" \
        "v_bitop3_b32 %[t], v115, %[f3], %[t] bitop3:0xbe\n\t" \
        "v_and_b32 %[t], 0xfff, %[t]\n\t" \
        "v_cmp_eq_u32 vcc, 0, %[t]\n\t" \
        "s_andn2_b64 %[p0], %[p0], vcc\n\t" \
        "s_mov_b64 exec, vcc\n\t" \
        "v_and_b32 %[o0], 0xfffff000, v112\n\t" \
        "v_and_b32 %[o1], 0xfffff000, v113\n\t" \
        "v_and_b32 %[o2], 0xfffff000, v114\n\t" \
        "v_and_b32 %[o3], 0xfffff000, v115\n\t" \
        "PS0_0_%=:\n\t" \
        "s_mov_b64 exec, %[p1]\n\t" \
        "s_cbranch_execz PS0_1_%=\n\t" \
        "v_xor_b32 %[t], %[f0], v116\n\t" \
        "v_bitop3_b32 %[t], v117, %[f1], %[t] bitop3:0xbe\n\t" \
        "v_bitop3_b32 %[t], v118, %[f2], %[t] bitop3:0xbe\n\t" \
        "v_bitop3_b32 %[t], v119, %[f3], %[t] bitop3:0xbe\n\t" \
        "v_and_b32 %[t], 0xfff, %[t]\n\t" \
        "v_cmp_eq_u32 vcc, 0, %[t]\n\t" \
        "s_andn2_b64 %[p1], %[p1], vcc\n\t" \
        "s_mov_b64 exec, vcc\n\t" \
        "v_and_b32 %[o4], 0xfffff000, v116\n\t" \
        "v_and_b32 %[o5], 0xfffff000, v117\n\t" \
        "v_and_b32 %[o6], 0xfffff000, v118\n\t" \
        "v_and_b32 %[o7], 0xfffff000, v119\n\t" \
        "PS0_1_%=:\n\t" \
        "s_mov_b64 exec, %[p2]\n\t" \
        "s_cbranch_execz PS0_2_%=\n\t" \
        "v_xor_b32 %[t], %[tag], v121\n\t" \
        "v_bitop3_b32 %[t], v123, %[tag], %[t] bitop3:0xbe\n\t" \
        "v_cmp_eq_u32 vcc, 0, %[t]\n\t" \
        "s_andn2_b64 %[p2], %[p2], vcc\n\t" \
        "s_mov_b64 exec, vcc\n\t" \
        "v_mov_b32 %[o8], v120\n\t" \
        "v_mov_b32 %[o9], v122\n\t" \
        "PS0_2_%=:\n\t" \
        "s_mov_b64 exec, %[p3]\n\t" \
        "s_cbranch_execz PS0_3_%=\n\t" \
        "v_xor_b32 %[t], %[tag], v125\n\t" \
        "v_bitop3_b32 %[t], v127, %[tag], %[t] bitop3:0xbe\n\t" \
        "v_cmp_eq_u32 vcc, 0, %[t]\n\t" \
        "s_andn2_b64 %[p3], %[p3], vcc\n\t" \
        "s_mov_b64 exec, vcc\n\t" \
        "v_mov_b32 %[o10], v124\n\t" \
        "v_mov_b32 %[o11], v126\n\t" \
        "PS0_3_%=:\n\t" \
        "s_or_b64 vcc, %[p0], %[p1]\n\t" \
        "s_or_b64 vcc, vcc, %[p2]\n\t" \
        "s_or_b64 vcc, vcc, %[p3]\n\t" \
        "s_cbranch_scc0 PE_%=\n\t" \
        "s_or_b64 exec, %[p0], 1\n\t" \
        "buffer_load_dwordx4 v[112:115], %[off], %[rs], 0 offen sc1\n\t" \
        "s_or_b64 exec, %[p1], 1\n\t" \
        "buffer_load_dwordx4 v[116:119], %[off], %[rs], %[s1] offen sc1\n\t" \
        "s_or_b64 exec, %[p2], 1\n\t" \
        "buffer_load_dwordx4 v[120:123], %[offm], %[rs], 0 offen sc1\n\t" \
        "s_or_b64 exec, %[p3], 1\n\t" \
        "buffer_load_dwordx4 v[124:127], %[offm], %[rs], 0 offen offset:1024 sc1\n\t" \
        "s_sub_u32 %[cnt], %[cnt], 1\n\t" \
        "s_cbranch_scc0 PL_%=\n\t" \
        "PE_%=:\n\t" \
        "s_mov_b64 exec, -1\n\t" \
        ""
#define POLLO_CLOB "v112", "v113", "v114", "v115", "v116", "v117", "v118", "v119", "v120", "v121", "v122", "v123", "v124", "v125", "v126", "v127"
#define POLLY_TEXT \
        "s_mov_b64 %[p0], -1\n\t" \
        "s_mov_b64 %[p1], -1\n\t" \
        "buffer_load_dwordx4 v[112:115], %[off], %[rs], 0 offen sc1\n\t" \
        "buffer_load_dwordx4 v[116:119], %[off], %[rs], %[s1] offen sc1\n\t" \
        "s_sleep 6\n\t" \
        "buffer_load_dwordx4 v[120:123], %[off], %[rs], 0 offen sc1\n\t" \
        "buffer_load_dwordx4 v[124:127], %[off], %[rs], %[s1] offen sc1\n\t" \
        "s_mov_b32 %[cnt], 32767\n\t" \
        "PL_%=:\n\t" \
        "s_waitcnt vmcnt(2)\n\t" \
        "s_mov_b64 exec, %[p0]\n\t" \
        "s_cbranch_execz PS0_0_%=\n\t" \
        "v_xor_b32 %[t], %[f0], v112\n\t" \
        "v_bitop3_b32 %[t], v113, %[f1], %[t] bitop3:0xbe\n\t" \
        "v_bitop3_b32 %[t], v114, %[f2], %[t] bitop3:0xbe\n\t" \
        "v_bitop3_b32 %[t], v115, %[f3], %[t] bitop3:0xbe\n\t" \
        "v_and_b32 %[t], 0xfff, %[t]\n\t" \
        "v_cmp_eq_u32 vcc, 0, %[t]\n\t" \
        "s_andn2_b64 %[p0], %[p0], vcc\n\t" \
        "s_mov_b64 exec, vcc\n\t" \
        "v_and_b32 %[o0], 0xfffff000, v112\n\t" \
        "v_and_b32 %[o1], 0xfffff000, v113\n\t" \
        "v_and_b32 %[o2], 0xfffff000, v114\n\t" \
        "v_and_b32 %[o3], 0xfffff000, v115\n\t" \
        "PS0_0_%=:\n\t" \
        "s_mov_b64 exec, %[p1]\n\t" \
        "s_cbranch_execz PS0_1_%=\n\t" \
        "v_xor_b32 %[t], %[f0], v116\n\t" \
        "v_bitop3_b32 %[t], v117, %[f1], %[t] bitop3:0xbe\n\t" \
        "v_bitop3_b32 %[t], v118, %[f2], %[t] bitop3:0xbe\n\t" \
        "v_bitop3_b32 %[t], v119, %[f3], %[t] bitop3:0xbe\n\t" \
        "v_and_b32 %[t], 0xfff, %[t]\n\t" \
        "v_cmp_eq_u32 vcc, 0, %[t]\n\t" \
        "s_andn2_b64 %[p1], %[p1], vcc\n\t" \
        "s_mov_b64 exec, vcc\n\t" \
        "v_and_b32 %[o4], 0xfffff000, v116\n\t" \
        "v_and_b32 %[o5], 0xfffff000, v117\n\t" \
        "v_and_b32 %[o6], 0xfffff000, v118\n\t" \
        "v_and_b32 %[o7], 0xfffff000, v119\n\t" \
        "PS0_1_%=:\n\t" \
        "s_or_b64 vcc, %[p0], %[p1]\n\t" \
        "s_cbranch_scc0 PE_%=\n\t" \
        "s_or_b64 exec, %[p0], 1\n\t" \
        "buffer_load_dwordx4 v[112:115], %[off], %[rs], 0 offen sc1\n\t" \
        "s_or_b64 exec, %[p1], 1\n\t" \
        "buffer_load_dwordx4 v[116:119], %[off], %[rs], %[s1] offen sc1\n\t" \
        "s_waitcnt vmcnt(2)\n\t" \
        "s_mov_b64 exec, %[p0]\n\t" \
        "s_cbranch_execz PS1_0_%=\n\t" \
        "v_xor_b32 %[t], %[f0], v120\n\t" \
        "v_bitop3_b32 %[t], v121, %[f1], %[t] bitop3:0xbe\n\t" \
        "v_bitop3_b32 %[t], v122, %[f2], %[t] bitop3:0xbe\n\t" \
        "v_bitop3_b32 %[t], v123, %[f3], %[t] bitop3:0xbe\n\t" \
        "v_and_b32 %[t], 0xfff, %[t]\n\t" \
        "v_cmp_eq_u32 vcc, 0, %[t]\n\t" \
        "s_andn2_b64 %[p0], %[p0], vcc\n\t" \
        "s_mov_b64 exec, vcc\n\t" \
        "v_and_b32 %[o0], 0xfffff000, v120\n\t" \
        "v_and_b32 %[o1], 0xfffff000, v121\n\t" \
        "v_and_b32 %[o2], 0xfffff000, v122\n\t" \
        "v_and_b32 %[o3], 0xfffff000, v123\n\t" \
        "PS1_0_%=:\n\t" \
        "s_mov_b64 exec, %[p1]\n\t" \
        "s_cbranch_execz PS1_1_%=\n\t" \
        "v_xor_b32 %[t], %[f0], v124\n\t" \
        "v_bitop3_b32 %[t], v125, %[f1], %[t] bitop3:0xbe\n\t" \
        "v_bitop3_b32 %[t], v126, %[f2], %[t] bitop3:0xbe\n\t" \
        "v_bitop3_b32 %[t], v127, %[f3], %[t] bitop3:0xbe\n\t" \
        "v_and_b32 %[t], 0xfff, %[t]\n\t" \
        "v_cmp_eq_u32 vcc, 0, %[t]\n\t" \
        "s_andn2_b64 %[p1], %[p1], vcc\n\t" \
        "s_mov_b64 exec, vcc\n\t" \
        "v_and_b32 %[o4], 0xfffff000, v124\n\t" \
        "v_and_b32 %[o5], 0xfffff000, v125\n\t" \
        "v_and_b32 %[o6], 0xfffff000, v126\n\t" \
        "v_and_b32 %[o7], 0xfffff000, v127\n\t" \
        "PS1_1_%=:\n\t" \
        "s_or_b64 vcc, %[p0], %[p1]\n\t" \
        "s_cbranch_scc0 PE_%=\n\t" \
        "s_or_b64 exec, %[p0], 1\n\t" \
        "buffer_load_dwordx4 v[120:123], %[off], %[rs], 0 offen sc1\n\t" \
        "s_or_b64 exec, %[p1], 1\n\t" \
        "buffer_load_dwordx4 v[124:127], %[off], %[rs], %[s1] offen sc1\n\t" \
        "s_sub_u32 %[cnt], %[cnt], 1\n\t" \
        "s_cbranch_scc0 PL_%=\n\t" \
        "PE_%=:\n\t" \
        "s_mov_b64 exec, -1\n\t" \
        ""
#define POLLY_CLOB "v112", "v113", "v114", "v115", "v116", "v117", "v118", "v119", "v120", "v121", "v122", "v123", "v124", "v125", "v126", "v127"

typedef int v4i __attribute__((ext_vector_type(4)));

constexpr unsigned LSPIN_CAP = 1u << 22;

__device__ __forceinline__ float dot2(unsigned a, unsigned b, float c) {
    return __builtin_amdgcn_fdot2_f32_bf16(__builtin_bit_cast(bf16x2, a), __builtin_bit_cast(bf16x2, b), c, false);
}
__device__ __forceinline__ unsigned pack_bf16(float lo, float hi) {
    bf16x2 v = {(__bf16)lo, (__bf16)hi};
    return __builtin_bit_cast(unsigned, v);
}
__device__ __forceinline__ unsigned perm_lo(unsigned a, unsigned b) { return __builtin_amdgcn_perm(b, a, 0x05040100u); }
__device__ __forceinline__ unsigned perm_hi(unsigned a, unsigned b) { return __builtin_amdgcn_perm(b, a, 0x07060302u); }
struct TagF { unsigned f0, f1, f2, f3; };
__device__ __forceinline__ TagF tag_fields(unsigned tag) { return TagF{tag & 0xfffu, (tag ^ (tag >> 12)) & 0xfffu, (tag ^ (tag >> 20)) & 0xfffu, (tag ^ (tag >> 6) ^ 0x555u) & 0xfffu}; }
__device__ __forceinline__ v4u pack_unit(float a, float b, float c, float d, const TagF& t) {
    return v4u{((__float_as_uint(a) + 0x800u) & 0xfffff000u) | t.f0, ((__float_as_uint(b) + 0x800u) & 0xfffff000u) | t.f1,
               ((__float_as_uint(c) + 0x800u) & 0xfffff000u) | t.f2, ((__float_as_uint(d) + 0x800u) & 0xfffff000u) | t.f3};
}
__device__ __forceinline__ float swap32_add(float a, float b) {
    auto t = __builtin_amdgcn_permlane32_swap(__float_as_uint(a), __float_as_uint(b), false, false);
    return __uint_as_float(t[0]) + __uint_as_float(t[1]);
}
__device__ __forceinline__ float swap16_add(float a, float b) {
    auto t = __builtin_amdgcn_permlane16_swap(__float_as_uint(a), __float_as_uint(b), false, false);
    return __uint_as_float(t[0]) + __uint_as_float(t[1]);
}
template <int CTRL> __device__ __forceinline__ float dpp(float x) {
    return __builtin_bit_cast(float, __builtin_amdgcn_mov_dpp(__builtin_bit_cast(int, x), CTRL, 0xf, 0xf, true));
}
constexpr int XOR1 = 0xB1, XOR2 = 0x4E, ROR4 = 0x124, ROR8 = 0x128;
__device__ __forceinline__ float xrow_max(float x) {
    auto s = __builtin_amdgcn_permlane16_swap(__float_as_uint(x), __float_as_uint(x), false, false);
    x = fmaxf(__uint_as_float(s[0]), __uint_as_float(s[1]));
    auto t = __builtin_amdgcn_permlane32_swap(__float_as_uint(x), __float_as_uint(x), false, false);
    return fmaxf(__uint_as_float(t[0]), __uint_as_float(t[1]));
}
__device__ __forceinline__ float xrow_sum(float x) {
    auto s = __builtin_amdgcn_permlane16_swap(__float_as_uint(x), __float_as_uint(x), false, false);
    x = __uint_as_float(s[0]) + __uint_as_float(s[1]);
    auto t = __builtin_amdgcn_permlane32_swap(__float_as_uint(x), __float_as_uint(x), false, false);
    return __uint_as_float(t[0]) + __uint_as_float(t[1]);
}
__device__ __forceinline__ f32x4 mfma16(v4u a, v4u b, f32x4 c) {
    return __builtin_amdgcn_mfma_f32_16x16x32_bf16(__builtin_bit_cast(bf16x8, a), __builtin_bit_cast(bf16x8, b), c, 0, 0, 0);
}
#define SB() __builtin_amdgcn_sched_barrier(0)
#define CFENCE() do { asm volatile("" ::: "memory"); SB(); } while (0)
#define BAR() do { asm volatile("s_waitcnt lgkmcnt(0)" ::: "memory"); __builtin_amdgcn_s_barrier(); CFENCE(); } while (0)
#define VMWAIT(n) do { SB(); asm volatile("s_waitcnt vmcnt(" #n ")" ::: "memory"); SB(); } while (0)

__device__ __forceinline__ void lds_signal(int word) {
    SB();
    asm volatile("s_waitcnt lgkmcnt(0)" ::: "memory");
    asm volatile("s_mov_b64 exec, 1\n\tds_add_u32 %0, %1\n\ts_mov_b64 exec, -1"
                 :: "v"(L_SYNC + word), "v"(1u) : "memory");
    SB();
}
template <int SLEEP> __device__ __forceinline__ void lds_wait(int word, unsigned want) {
    unsigned v_, s_, cnt_;
    SB();
    asm volatile("s_mov_b32 %[cnt], %[cap]\n\t"
                 "LW_%=:\n\t"
                 "ds_read_b32 %[v], %[a]\n\t"
                 "s_waitcnt lgkmcnt(0)\n\t"
                 "v_readfirstlane_b32 %[s], %[v]\n\t"
                 "s_cmp_ge_u32 %[s], %[want]\n\t"
                 "s_cbranch_scc1 LD_%=\n\t"
                 "s_sleep %[sl]\n\t"
                 "s_sub_u32 %[cnt], %[cnt], 1\n\t"
                 "s_cbranch_scc0 LW_%=\n\t"
                 "LD_%=:"
                 : [v] "=&v"(v_), [s] "=&s"(s_), [cnt] "=&s"(cnt_)
                 : [a] "v"(L_SYNC + word), [want] "s"(want), [sl] "n"(SLEEP), [cap] "s"(LSPIN_CAP) : "scc", "memory");
    SB();
}

__device__ __forceinline__ unsigned lds_peek(int word) {
    unsigned v_, s_;
    SB();
    asm volatile("ds_read_b32 %[v], %[a]\n\ts_waitcnt lgkmcnt(0)\n\tv_readfirstlane_b32 %[s], %[v]" : [v] "=&v"(v_), [s] "=s"(s_) : [a] "v"(L_SYNC + word) : "memory");
    SB();
    return s_;
}

#define FINISH() __builtin_amdgcn_endpgm()

__global__ void __launch_bounds__(NTHR, 1)
fused_decode_kernel(const unsigned short* __restrict__ xA, const unsigned short* __restrict__ kA, const unsigned short* __restrict__ vA,
                    const unsigned short* __restrict__ wqA, const unsigned short* __restrict__ woA,
                    const unsigned short* __restrict__ xB, const unsigned short* __restrict__ kB, const unsigned short* __restrict__ vB,
                    const unsigned short* __restrict__ wqB, const unsigned short* __restrict__ woB,
                    unsigned short* __restrict__ out, unsigned char* __restrict__ ws)
{
    extern __shared__ __attribute__((aligned(16))) unsigned char smem[];
    LAS unsigned char* L = (LAS unsigned char*)smem;
    const int tid = threadIdx.x, lane = tid & 63;
    const int w = __builtin_amdgcn_readfirstlane(tid >> 6);
    const int bid = blockIdx.x;
    const int kvh = bid & 7, b = (bid >> 3) & 1, s = bid >> 4;
    const int grp = b * 8 + kvh;
    const int g = lane >> 4, c = lane & 15;

    typedef unsigned long long u64;
    u64 xA_ = (u64)xA, kA_ = (u64)kA, vA_ = (u64)vA, wqA_ = (u64)wqA, woA_ = (u64)woA;
    u64 xB_ = (u64)xB, kB_ = (u64)kB, vB_ = (u64)vB, wqB_ = (u64)wqB, woB_ = (u64)woB, out_ = (u64)out, ws_ = (u64)ws;
    asm volatile("" : "+s"(xA_), "+s"(kA_), "+s"(vA_), "+s"(wqA_), "+s"(woA_), "+s"(xB_), "+s"(kB_), "+s"(vB_), "+s"(wqB_), "+s"(woB_), "+s"(out_), "+s"(ws_));
    const bool wide = (s >> 2) == 1;
    GAS unsigned char* const wsg = (GAS unsigned char*)ws_;
    const __amdgpu_buffer_rsrc_t rws = __builtin_amdgcn_make_buffer_rsrc((void*)wsg, 0, (int)WS_END, RSRC_W3);

    if (w >= WKV0) {
        const int kw = w - WKV0;
        const u64 kv_off = ((size_t)kvh * SK + (SK - WIN) + KEYS_WG * s) * (DH * 2);
        const u64 kp_ = (b ? kB_ : kA_) + kv_off, vp_ = (b ? vB_ : vA_) + kv_off;
        const v4i rv = {(int)(unsigned)vp_, (int)((unsigned)(vp_ >> 32) & 0xffffu), KEYS_WG * DH * 2, RSRC_W3};
        const v4i rk = {(int)(unsigned)kp_, (int)((unsigned)(kp_ >> 32) & 0xffffu), KEYS_WG * DH * 2, RSRC_W3};
        const int vov = (32 * kw + 4 * g) * 256 + c * 16;
        const int vok = (32 * kw + g) * 256, xk = (c ^ g) * 16;
        const unsigned kt = L_KT + kw * 8192;
        BAR();
#define K_DMA4(M0V, SOV) do { unsigned keep; \
            asm volatile("s_mov_b32 %[keep], m0\n\t" \
                         "s_mov_b32 m0, %[ma]\n\ts_nop 0\n\t" \
                         "buffer_load_dwordx4 %[v0], %[rk], %[so] offen " KV_MOD " lds\n\t" \
                         "buffer_load_dwordx4 %[v1], %[rk], %[so] offen offset:1024 " KV_MOD " lds\n\t" \
                         "buffer_load_dwordx4 %[v2], %[rk], %[so] offen offset:2048 " KV_MOD " lds\n\t" \
                         "buffer_load_dwordx4 %[v3], %[rk], %[so] offen offset:3072 " KV_MOD " lds\n\t" \
                         "s_mov_b32 m0, %[keep]" \
                         : [keep] "=&s"(keep) \
                         : [ma] "s"(M0V), [so] "s"(SOV), [rk] "s"(rk), \
                           [v0] "v"(vok + xk), [v1] "v"(vok + (xk ^ 64)), [v2] "v"(vok + (xk ^ 128)), [v3] "v"(vok + (xk ^ 192)) \
                         : "memory"); SB(); } while (0)
        v4u vf[8];
#define V_LOAD2(A, B, SO, OA, OB) \
            asm volatile("buffer_load_dwordx4 %0, %2, %3, %4 offen offset:" #OA " " KV_MOD "\n\t" \
                         "buffer_load_dwordx4 %1, %2, %3, %4 offen offset:" #OB " " KV_MOD \
                         : "=&v"(vf[A]), "=&v"(vf[B]) : "v"(vov), "s"(rv), "s"(SO) : "memory"); SB()
#define V_WAIT(N, A, B, C, D) do { SB(); asm volatile("s_waitcnt vmcnt(" #N ")" : "+v"(vf[A]), "+v"(vf[B]), "+v"(vf[C]), "+v"(vf[D]) :: "memory"); } while (0)
        K_DMA4(kt, 0);
        K_DMA4(kt + 4096, 4096);
        lds_wait<1>(S_QPUB, 1);
        V_LOAD2(0, 1, 0, 0, 256); V_LOAD2(2, 3, 0, 512, 768); V_LOAD2(4, 5, 4096, 0, 256); V_LOAD2(6, 7, 4096, 512, 768);
        v4u qb[4], kf[2][4], bv[8], pa;
#define K_FRAGS() do { _Pragma("unroll") for (int t = 0; t < 4; ++t) _Pragma("unroll") for (int u = 0; u < 2; ++u) \
            kf[u][t] = *(LAS v4u*)(L + kt + u * 4096 + c * 256 + (xk ^ (t * 64))); \
            asm volatile("" : "+v"(kf[0][0]), "+v"(kf[0][1]), "+v"(kf[0][2]), "+v"(kf[0][3]), "+v"(kf[1][0]), "+v"(kf[1][1]), "+v"(kf[1][2]), "+v"(kf[1][3])); SB(); } while (0)
#define V_FRAGS(J0) do { _Pragma("unroll") for (int i = 0; i < 8; ++i) _Pragma("unroll") for (int jj = J0; jj < J0 + 2; ++jj) \
            bv[i][jj] = (i & 1) ? perm_hi(vf[2 * jj][i >> 1], vf[2 * jj + 1][i >> 1]) : perm_lo(vf[2 * jj][i >> 1], vf[2 * jj + 1][i >> 1]); \
            SB(); } while (0)
#define V_PIN() asm volatile("" : "+v"(bv[0]), "+v"(bv[1]), "+v"(bv[2]), "+v"(bv[3]), "+v"(bv[4]), "+v"(bv[5]), "+v"(bv[6]), "+v"(bv[7]))
#define QK_SOFTMAX() do { \
            _Pragma("unroll") for (int t = 0; t < 4; ++t) qb[t] = *(LAS v4u*)(L + L_QFULL + (c >> 2) * QF_STRIDE + (32 * t + 8 * g) * 2); \
            f32x4 sc[2]; \
            _Pragma("unroll") for (int u = 0; u < 2; ++u) { sc[u] = f32x4{0.f, 0.f, 0.f, 0.f}; \
                _Pragma("unroll") for (int t = 0; t < 4; ++t) sc[u] = mfma16(kf[u][t], qb[t], sc[u]); } \
            float m = fmaxf(fmaxf(fmaxf(sc[0][0], sc[0][1]), fmaxf(sc[0][2], sc[0][3])), fmaxf(fmaxf(sc[1][0], sc[1][1]), fmaxf(sc[1][2], sc[1][3]))); \
            m = xrow_max(m) * SM_SCALE_LOG2; \
            float p[8]; \
            _Pragma("unroll") for (int u = 0; u < 2; ++u) _Pragma("unroll") for (int r = 0; r < 4; ++r) \
                p[4 * u + r] = __builtin_amdgcn_exp2f(fmaf(sc[u][r], SM_SCALE_LOG2, -m)); \
            float ls = ((p[0] + p[1]) + (p[2] + p[3])) + ((p[4] + p[5]) + (p[6] + p[7])); \
            ls = xrow_sum(ls); \
            if ((lane & 0x33) == 0) { *(LAS float*)(L + L_MX + ((c >> 2) * 8 + kw) * 4) = m; *(LAS float*)(L + L_LS + ((c >> 2) * 8 + kw) * 4) = ls; } \
            lds_signal(S_ML); \
            pa = v4u{pack_bf16(p[0], p[1]), pack_bf16(p[2], p[3]), pack_bf16(p[4], p[5]), pack_bf16(p[6], p[7])}; \
            asm volatile("" : "+v"(pa)); SB(); } while (0)
        VMWAIT(8);
        K_FRAGS();
        const bool q_early = lds_peek(S_FQ) != 0;
        if (q_early) {
            QK_SOFTMAX();
        }
        V_WAIT(4, 0, 1, 2, 3); V_FRAGS(0);
        V_WAIT(0, 4, 5, 6, 7); V_FRAGS(2);
        V_PIN();
        if (!q_early) {
            lds_wait<1>(S_FQ, 1);
            QK_SOFTMAX();
        }
        f32x4 oacc[8];
#pragma unroll
        for (int i = 0; i < 8; ++i) oacc[i] = mfma16(pa, bv[i], f32x4{0.f, 0.f, 0.f, 0.f});
        *(LAS f32x4*)(L + L_OPART + kw * OP_WS + lane * 16) = f32x4{oacc[0][0], oacc[1][0], oacc[2][0], oacc[3][0]};
        *(LAS f32x4*)(L + L_OPART + kw * OP_WS + OP_HS + lane * 16) = f32x4{oacc[4][0], oacc[5][0], oacc[6][0], oacc[7][0]};
        lds_signal(S_O);
        FINISH();
    } else if (w == WCTL) {
        const unsigned long long t_start = __builtin_amdgcn_s_memrealtime();
        if (lane < 7) *(LAS unsigned*)(L + L_SYNC + lane * 4) = 0u;
        asm volatile("s_waitcnt lgkmcnt(0)" ::: "memory");
        SB();
        const unsigned epoch = __hip_atomic_load((GAS unsigned*)(wsg + WS_EPOCH + b * 256), __ATOMIC_RELAXED, __HIP_MEMORY_SCOPE_AGENT);
        SB();
        __builtin_amdgcn_s_barrier();
        CFENCE();
        __builtin_amdgcn_s_setprio(3);
        const v4i rsw = {(int)(unsigned)ws_, (int)((unsigned)(ws_ >> 32) & 0xffffu), (int)WS_END, RSRC_W3};
        const unsigned tag = __builtin_amdgcn_readfirstlane(epoch) + 1u;
        if (lane == 0) *(LAS unsigned*)(L + L_SYNC + S_TAG) = tag;
        GAS unsigned short* const outg = (GAS unsigned short*)out_;
        const TagF tf = tag_fields(tag);
        lds_wait<0>(S_QP, NWW);
        {
            f32x4 v = {0.f, 0.f, 0.f, 0.f};
#pragma unroll
            for (int ww = 0; ww < NWW; ++ww) v += *(LAS f32x4*)(L + L_QPART + ww * 512 + (lane & 31) * 16);
            if (lane < (wide ? 32 : 16)) __builtin_amdgcn_raw_buffer_store_b128(pack_unit(v[0], v[1], v[2], v[3], tf), rws, WS_Q + (grp * 16 + s) * 512 + lane * 16, 0, AUX_SC1);
        }
        SB();
        lds_signal(S_QPUB);
        for (int i = 0; i < 4096 && __builtin_amdgcn_s_memrealtime() - t_start < Q_AT; ++i) __builtin_amdgcn_s_sleep(1);
        {
            float q_[20]; unsigned t_, cnt_; unsigned long long p0_, p1_, p2_, p3_, p4_;
            asm volatile(POLLQ_TEXT : [o0] "=&v"(q_[0]), [o1] "=&v"(q_[1]), [o2] "=&v"(q_[2]), [o3] "=&v"(q_[3]), [o4] "=&v"(q_[4]), [o5] "=&v"(q_[5]),
                                      [o6] "=&v"(q_[6]), [o7] "=&v"(q_[7]), [o8] "=&v"(q_[8]), [o9] "=&v"(q_[9]), [o10] "=&v"(q_[10]), [o11] "=&v"(q_[11]),
                                      [o12] "=&v"(q_[12]), [o13] "=&v"(q_[13]), [o14] "=&v"(q_[14]), [o15] "=&v"(q_[15]),
                                      [o16] "=&v"(q_[16]), [o17] "=&v"(q_[17]), [o18] "=&v"(q_[18]), [o19] "=&v"(q_[19]), [t] "=&v"(t_), [cnt] "=&s"(cnt_),
                                      [p0] "=&s"(p0_), [p1] "=&s"(p1_), [p2] "=&s"(p2_), [p3] "=&s"(p3_), [p4] "=&s"(p4_)
                         : [off] "v"(WS_Q + grp * 8192 + g * 512 + c * 16), [off1] "v"(WS_Q + grp * 8192 + (4 + (lane >> 5)) * 512 + (lane & 31) * 16),
                           [rs] "s"(rsw), [s1] "s"(8 * 512), [s2] "s"(12 * 512), [f0] "s"(tf.f0), [f1] "s"(tf.f1), [f2] "s"(tf.f2), [f3] "s"(tf.f3)
                         : "vcc", "scc", "memory", POLLQ_CLOB);
#pragma unroll
            for (int k = 0; k < 3; ++k) {
                const float e0 = swap16_add(q_[4 * k], q_[4 * k]), e1 = swap16_add(q_[4 * k + 1], q_[4 * k + 1]);
                const float e2 = swap16_add(q_[4 * k + 2], q_[4 * k + 2]), e3 = swap16_add(q_[4 * k + 3], q_[4 * k + 3]);
                if ((g & 1) == 0) *(LAS v2u*)(L + L_QFULL + (k ? k + 1 : 0) * QF_STRIDE + (64 * (g >> 1) + 4 * c) * 2) = v2u{pack_bf16(e0, e1), pack_bf16(e2, e3)};
            }
            {
                const float e0 = q_[12] + q_[16], e1 = q_[13] + q_[17], e2 = q_[14] + q_[18], e3 = q_[15] + q_[19];
                const v2u qq = {pack_bf16(swap32_add(e0, e0), swap32_add(e1, e1)), pack_bf16(swap32_add(e2, e2), swap32_add(e3, e3))};
                if (lane < 32) *(LAS v2u*)(L + L_QFULL + QF_STRIDE + lane * 8) = qq;
            }
        }
        lds_signal(S_FQ);
        lds_wait<1>(S_ML, NKV);
        {
            const int hh = c & 3;
            const f32x4 m0 = *(LAS f32x4*)(L + L_MX + hh * 32), m1 = *(LAS f32x4*)(L + L_MX + hh * 32 + 16);
            const f32x4 l0 = *(LAS f32x4*)(L + L_LS + hh * 32), l1 = *(LAS f32x4*)(L + L_LS + hh * 32 + 16);
            const float M = fmaxf(fmaxf(fmaxf(m0[0], m0[1]), fmaxf(m0[2], m0[3])), fmaxf(fmaxf(m1[0], m1[1]), fmaxf(m1[2], m1[3])));
            float ll = 0.f;
#pragma unroll
            for (int k = 0; k < 4; ++k) { ll = fmaf(__builtin_amdgcn_exp2f(m0[k] - M), l0[k], ll); ll = fmaf(__builtin_amdgcn_exp2f(m1[k] - M), l1[k], ll); }
            const v4u unit = {__float_as_uint(M), tag, __float_as_uint(ll), tag};
            if (lane < 8) __builtin_amdgcn_raw_buffer_store_b128(unit, rws, WS_ML + (grp * 16 + s) * 128 + lane * 16, 0, AUX_SC1);
        }
        SB();
        lds_wait<1>(S_PUB, NWW);
        __builtin_amdgcn_s_sleep(O_DELAY);
        {
            float o_[12]; unsigned t_, cnt_; unsigned long long p0_, p1_, p2_, p3_;
            asm volatile(POLLO_TEXT : [o0] "=&v"(o_[0]), [o1] "=&v"(o_[1]), [o2] "=&v"(o_[2]), [o3] "=&v"(o_[3]), [o4] "=&v"(o_[4]), [o5] "=&v"(o_[5]),
                                      [o6] "=&v"(o_[6]), [o7] "=&v"(o_[7]), [o8] "=&v"(o_[8]), [o9] "=&v"(o_[9]), [o10] "=&v"(o_[10]), [o11] "=&v"(o_[11]),
                                      [t] "=&v"(t_), [cnt] "=&s"(cnt_), [p0] "=&s"(p0_), [p1] "=&s"(p1_), [p2] "=&s"(p2_), [p3] "=&s"(p3_)
                         : [off] "v"(WS_O + grp * 32768 + (lane >> 3) * 2048 + (8 * s + (lane & 7)) * 16), [rs] "s"(rsw),
                           [offm] "v"(WS_ML + grp * 2048 + (lane >> 3) * 128 + (s >> 2) * 16), [tag] "s"(tag),
                           [f0] "s"(tf.f0), [f1] "s"(tf.f1), [f2] "s"(tf.f2), [f3] "s"(tf.f3), [s1] "s"(16384) : "vcc", "scc", "memory", POLLO_CLOB);
            float M = fmaxf(o_[8], o_[10]);
            M = fmaxf(M, dpp<ROR8>(M));
            M = xrow_max(M);
            const float w0 = __builtin_amdgcn_exp2f(o_[8] - M), w1 = __builtin_amdgcn_exp2f(o_[10] - M);
            float den = fmaf(w0, o_[9], w1 * o_[11]);
            den += dpp<ROR8>(den);
            const float inv = __builtin_amdgcn_rcpf(xrow_sum(den));
            float n[4];
#pragma unroll
            for (int e = 0; e < 4; ++e) n[e] = fmaf(w0, o_[e], w1 * o_[4 + e]) * inv;
            float r = swap16_add(swap32_add(n[0], n[2]), swap32_add(n[1], n[3]));
            r += dpp<ROR8>(r);
            if ((lane & 8) == 0) *(LAS unsigned short*)(L + L_OFIN + (4 * (lane & 7) + g) * 2) = (unsigned short)pack_bf16(r, r);
        }
        lds_signal(S_FO);
        {
            const int idx = kvh * 16 + s;
            __builtin_amdgcn_s_sleep(Y_DELAY);
            float o_[8]; unsigned t_, cnt_; unsigned long long p0_, p1_;
            asm volatile(POLLY_TEXT : [o0] "=&v"(o_[0]), [o1] "=&v"(o_[1]), [o2] "=&v"(o_[2]), [o3] "=&v"(o_[3]), [o4] "=&v"(o_[4]), [o5] "=&v"(o_[5]),
                                      [o6] "=&v"(o_[6]), [o7] "=&v"(o_[7]), [t] "=&v"(t_), [cnt] "=&s"(cnt_), [p0] "=&s"(p0_), [p1] "=&s"(p1_)
                         : [off] "v"(WS_Y + b * (128 * 2048) + lane * 2048 + idx * 16), [rs] "s"(rsw),
                           [f0] "s"(tf.f0), [f1] "s"(tf.f1), [f2] "s"(tf.f2), [f3] "s"(tf.f3), [s1] "s"(131072) : "vcc", "scc", "memory", POLLY_CLOB);
            if (idx == 0 && lane == 0) __hip_atomic_store((GAS unsigned*)(wsg + WS_EPOCH + b * 256), tag, __ATOMIC_RELAXED, __HIP_MEMORY_SCOPE_AGENT);
            SB();
            float r = swap16_add(swap32_add(o_[0] + o_[4], o_[2] + o_[6]), swap32_add(o_[1] + o_[5], o_[3] + o_[7]));
            r += dpp<ROR8>(r); r += dpp<ROR4>(r); r += dpp<XOR2>(r); r += dpp<XOR1>(r);
            if (c == 0) outg[b * 512 + 4 * idx + g] = (unsigned short)pack_bf16(r, r);
        }
        FINISH();
    } else {
        const int qrow0 = wide ? 128 * (s & 3) : 256 * (s & 1), qcol0 = kvh * 512 + (wide ? 128 : 64 * (s >> 1));
        GAS unsigned char* const x = (GAS unsigned char*)((b ? xB_ : xA_) + qrow0 * 2);
        GAS unsigned char* const wqp = (GAS unsigned char*)((b ? wqB_ : wqA_) + (size_t)qrow0 * (NQ * 2) + qcol0 * 2);
        const __amdgpu_buffer_rsrc_t rx = __builtin_amdgcn_make_buffer_rsrc((void*)x, 0, 256 * 2, RSRC_W3);
        const __amdgpu_buffer_rsrc_t rq = __builtin_amdgcn_make_buffer_rsrc((void*)wqp, 0, 255 * NQ * 2 + 256, RSRC_W3);
        const int qrow = wide ? 32 * w + 8 * g : 64 * w + 8 * (lane >> 3);
        const int voq = qrow * (NQ * 2) + (wide ? c : (lane & 7)) * 16;
        const v4u xw = __builtin_amdgcn_raw_buffer_load_b128(rx, qrow * 2, 0, 0);
        v4u wq[8];
#pragma unroll
        for (int i = 0; i < 8; ++i) wq[i] = __builtin_amdgcn_raw_buffer_load_b128(rq, voq, i * (NQ * 2), AUX_NT);
        SB();
        BAR();
        float a[8];
#pragma unroll
        for (int e = 0; e < 8; ++e) a[e] = 0.f;
#pragma unroll
        for (int h = 0; h < 4; ++h)
#pragma unroll
            for (int k = 0; k < 4; ++k) {
                a[2 * k] = dot2(perm_lo(wq[2 * h][k], wq[2 * h + 1][k]), xw[h], a[2 * k]);
                a[2 * k + 1] = dot2(perm_hi(wq[2 * h][k], wq[2 * h + 1][k]), xw[h], a[2 * k + 1]);
            }
        float r2[2];
#pragma unroll
        for (int e = 0; e < 2; ++e) {
            float v = swap16_add(swap32_add(a[e], a[e + 4]), swap32_add(a[e + 2], a[e + 6]));
            const float vr = dpp<ROR8>(v);
            r2[e] = wide ? v : v + vr;
        }
        if (wide || c < 8) *(LAS f32x2*)(L + L_QPART + w * 512 + (c * 8 + 2 * g) * 4) = f32x2{r2[0], r2[1]};
        lds_signal(S_QP);
        lds_wait<1>(S_ML, NKV);
        const unsigned tag = __builtin_amdgcn_readfirstlane(*(LAS unsigned*)(L + L_SYNC + S_TAG));
        const TagF tf = tag_fields(tag);
        const int hi = lane >> 5, ll = lane & 31;
        float f[4];
        {
            const f32x4 m0 = *(LAS f32x4*)(L + L_MX + w * 32), m1 = *(LAS f32x4*)(L + L_MX + w * 32 + 16);
            const float M = fmaxf(fmaxf(fmaxf(m0[0], m0[1]), fmaxf(m0[2], m0[3])), fmaxf(fmaxf(m1[0], m1[1]), fmaxf(m1[2], m1[3])));
#pragma unroll
            for (int k = 0; k < 4; ++k) f[k] = __builtin_amdgcn_exp2f((hi ? m1[k] : m0[k]) - M);
        }
        asm volatile("" : "+v"(f[0]), "+v"(f[1]), "+v"(f[2]), "+v"(f[3]));
        SB();
        lds_wait<0>(S_O, NKV);
        {
            const int off = L_OPART + hi * (4 * OP_WS) + (ll & 1) * OP_HS + (16 * w + (ll >> 1)) * 16;
            f32x4 v = {0.f, 0.f, 0.f, 0.f};
#pragma unroll
            for (int k = 0; k < 4; ++k) { const f32x4 o = *(LAS f32x4*)(L + off + k * OP_WS); v += f[k] * o; }
            const v4u unit = pack_unit(swap32_add(v[0], v[0]), swap32_add(v[1], v[1]), swap32_add(v[2], v[2]), swap32_add(v[3], v[3]), tf);
            if (lane < 32) __builtin_amdgcn_raw_buffer_store_b128(unit, rws, WS_O + (grp * 16 + s) * 2048 + (32 * w + ll) * 16, 0, AUX_SC1);
        }
        SB();
        lds_signal(S_PUB);
        GAS unsigned char* const wop = (GAS unsigned char*)((b ? woB_ : woA_) + (size_t)(kvh * 512 + 32 * s) * (DM * 2));
        const __amdgpu_buffer_rsrc_t ro = __builtin_amdgcn_make_buffer_rsrc((void*)wop, 0, 32 * DM * 2, RSRC_W3);
        v4u wo[8];
        const int voo = g * 8192 + w * 256 + c * 16;
#pragma unroll
        for (int j = 0; j < 8; ++j) wo[j] = __builtin_amdgcn_raw_buffer_load_b128(ro, voo + (j & 3) * 1024, (j >> 2) * 4096, AUX_NT);
        SB();
        v4u wb[8];
#pragma unroll
        for (int i = 0; i < 8; ++i)
#pragma unroll
            for (int jj = 0; jj < 4; ++jj)
                wb[i][jj] = (i & 1) ? perm_hi(wo[2 * jj][i >> 1], wo[2 * jj + 1][i >> 1]) : perm_lo(wo[2 * jj][i >> 1], wo[2 * jj + 1][i >> 1]);
        asm volatile("" : "+v"(wb[0]), "+v"(wb[1]), "+v"(wb[2]), "+v"(wb[3]), "+v"(wb[4]), "+v"(wb[5]), "+v"(wb[6]), "+v"(wb[7]));
        lds_wait<0>(S_FO, 1);
        const v4u oa = *(LAS v4u*)(L + L_OFIN + g * 16);
        f32x4 y[8];
#pragma unroll
        for (int i = 0; i < 8; ++i) y[i] = mfma16(oa, wb[i], f32x4{0.f, 0.f, 0.f, 0.f});
        {
            const v4u unit = pack_unit(g ? y[4][0] : y[0][0], g ? y[5][0] : y[1][0], g ? y[6][0] : y[2][0], g ? y[7][0] : y[3][0], tf);
            if (lane < 32) __builtin_amdgcn_raw_buffer_store_b128(unit, rws, WS_Y + (b * 128 + kvh * 16 + s) * 2048 + (32 * w + 2 * c + g) * 16, 0, AUX_SC1);
        }
        FINISH();
    }
}

extern "C" void kernel_launch(void* const* d_in, const int* in_sizes, int n_in, void* d_out, int out_size, void* d_ws, size_t ws_size,
                              hipStream_t stream) {
    (void)in_sizes; (void)n_in; (void)out_size;
    typedef const unsigned short* P;
    static_assert(WS_END <= (2u << 20), "workspace");
    unsigned char* const ws = (unsigned char*)d_ws + ((ws_size - (2u << 20)) & ~(size_t)4095);
    (void)hipFuncSetAttribute((const void*)fused_decode_kernel, hipFuncAttributeMaxDynamicSharedMemorySize, LDS_BYTES);
    fused_decode_kernel<<<256, NTHR, LDS_BYTES, stream>>>((P)d_in[0], (P)d_in[1], (P)d_in[2], (P)d_in[3], (P)d_in[4],
                                                         (P)d_in[5], (P)d_in[6], (P)d_in[7], (P)d_in[8], (P)d_in[9],
                                                         (unsigned short*)d_out, ws);
}
```
